```python
import torch

def _softmax(x, axis=-1):
    return torch.softmax(x, dim=axis)
def _gelu(x):
    return torch.nn.functional.gelu(x, approximate="tanh")
def _silu(x):
    return torch.nn.functional.silu(x)
def _sigmoid(x):
    return torch.sigmoid(x)
def _layernorm(x, axis=-1, eps=1e-5):
    m = x.mean(dim=axis, keepdim=True)
    v = x.var(dim=axis, keepdim=True, unbiased=False)
    return (x - m) / torch.sqrt(v + eps)
def _scatter_add(tgt, idx, src, axis=0):
    return tgt.clone().index_add_(axis, idx, src)
def _segment_sum(src, seg, n):
    out = torch.zeros((n,) + src.shape[1:], dtype=src.dtype)
    return out.index_add_(0, seg, src)
def _pad(x, pw):
    flat = tuple(v for pair in reversed(pw) for v in pair)
    return torch.nn.functional.pad(x, flat)

kernel_name = 'standalone__groupnorm__sampling'

def setup_inputs():

    _w = (lambda a: (lambda t: t.to(torch.bfloat16) if t.is_floating_point() else t)(torch.as_tensor(a)))
    B = 32
    G = 32
    cpg = max(1, 1024 // G)
    C = G * cpg
    H, W = 32, 16
    return {
        "x": _w(abs((lambda *s: torch.randn(*s, dtype=torch.bfloat16) * 0.05)(B, C, H, W))),
        "g": _w((lambda *s: torch.randn(*s, dtype=torch.bfloat16) * 0.05)(C) + 1.0),
        "b": _w((lambda *s: torch.randn(*s, dtype=torch.bfloat16) * 0.4)(C)),
    }

def reference(x, g, b):
    G = 32
    B, C, H, W = x.shape[0], x.shape[1], x.shape[2], x.shape[3]
    xr = x.reshape(B, G, C // G, H, W)
    mu = xr.mean(axis=(2, 3, 4), keepdims=True)
    var = ((xr - mu) ** 2).mean(axis=(2, 3, 4), keepdims=True)
    xn = (xr - mu) / torch.sqrt(var + 1e-5)
    xn = xn.reshape(B, C, H, W)
    return xn * g[None, :, None, None] + b[None, :, None, None]
```

```cpp
#include <hip/hip_runtime.h>
#include <hip/hip_bf16.h>
#include <stdint.h>

typedef unsigned int u32x4 __attribute__((ext_vector_type(4)));
typedef float f32x2 __attribute__((ext_vector_type(2)));
typedef __bf16 bf16x2 __attribute__((ext_vector_type(2)));

#define TPB 256
#define LD_AUX 0
#define ST_AUX 0

template <int CTRL, int ROWMASK>
__device__ __forceinline__ float dpp_add(float v) {
    int t = __builtin_amdgcn_update_dpp(0, __float_as_int(v), CTRL, ROWMASK, 0xf, false);
    return v + __int_as_float(t);
}
__device__ __forceinline__ float wave_sum(float v) {
    v = dpp_add<0xB1, 0xf>(v);
    v = dpp_add<0x4E, 0xf>(v);
    v = dpp_add<0x141, 0xf>(v);
    v = dpp_add<0x140, 0xf>(v);
    v = dpp_add<0x142, 0xa>(v);
    v = dpp_add<0x143, 0xc>(v);
    return __int_as_float(__builtin_amdgcn_readlane(__float_as_int(v), 63));
}
__device__ __forceinline__ float bf_lo(uint32_t u) { return __uint_as_float(u << 16); }
__device__ __forceinline__ float bf_hi(uint32_t u) { return __uint_as_float(u & 0xffff0000u); }
__device__ __forceinline__ uint32_t pack_bf16(float a, float b) {
    f32x2 f = {a, b};
    bf16x2 h = __builtin_convertvector(f, bf16x2);
    return __builtin_bit_cast(uint32_t, h);
}

constexpr int GROUP_ELEMS = 16384;
constexpr int NW = TPB / 64;
constexpr int NV = GROUP_ELEMS / 8 / TPB;

__global__ __launch_bounds__(TPB) void gn_kernel(const void* __restrict__ x, const uint16_t* __restrict__ gamma,
                                                 const uint16_t* __restrict__ beta, void* __restrict__ y) {
    __shared__ f32x2 red[NW];
    const int grp = blockIdx.x;
    const int lane = threadIdx.x & 63;
    const int wid = __builtin_amdgcn_readfirstlane(threadIdx.x >> 6);
    const __amdgpu_buffer_rsrc_t rx = __builtin_amdgcn_make_buffer_rsrc((char*)x + (size_t)grp * (GROUP_ELEMS * 2), 0, GROUP_ELEMS * 2, 0x00020000);
    const __amdgpu_buffer_rsrc_t ry = __builtin_amdgcn_make_buffer_rsrc((char*)y + (size_t)grp * (GROUP_ELEMS * 2), 0, GROUP_ELEMS * 2, 0x00020000);
    const int voff = lane * 16;
    const int soff = wid * (NV * 1024);
    u32x4 v[NV];
#pragma unroll
    for (int i = 0; i < NV; ++i) v[i] = __builtin_amdgcn_raw_buffer_load_b128(rx, voff + i * 1024, soff, LD_AUX);
    const int c0 = (grp & 31) * 32 + wid * NV;
    float gm[NV], bt[NV];
#pragma unroll
    for (int i = 0; i < NV; ++i) {
        gm[i] = __uint_as_float((uint32_t)gamma[c0 + i] << 16);
        bt[i] = __uint_as_float((uint32_t)beta[c0 + i] << 16);
    }
    float f[NV][8];
    float s0 = 0.f, s1 = 0.f;
#pragma unroll
    for (int i = 0; i < NV; ++i) {
#pragma unroll
        for (int j = 0; j < 4; ++j) {
            f[i][2 * j] = bf_lo(v[i][j]);
            f[i][2 * j + 1] = bf_hi(v[i][j]);
            s0 += f[i][2 * j];
            s1 += f[i][2 * j + 1];
        }
    }
    const float mw = wave_sum(s0 + s1) * (1.0f / (NV * 512));
    float q0 = 0.f, q1 = 0.f;
#pragma unroll
    for (int i = 0; i < NV; ++i) {
#pragma unroll
        for (int j = 0; j < 4; ++j) {
            f[i][2 * j] -= mw;
            f[i][2 * j + 1] -= mw;
            q0 = fmaf(f[i][2 * j], f[i][2 * j], q0);
            q1 = fmaf(f[i][2 * j + 1], f[i][2 * j + 1], q1);
        }
    }
    const float m2w = wave_sum(q0 + q1);
    if (lane == 0) red[wid] = f32x2{mw, m2w};
    __syncthreads();
    float mean = 0.f, m2 = 0.f;
#pragma unroll
    for (int w = 0; w < NW; ++w) { mean += red[w].x; m2 += red[w].y; }
    mean *= (1.0f / NW);
    float dv = 0.f;
#pragma unroll
    for (int w = 0; w < NW; ++w) { float d = red[w].x - mean; dv = fmaf(d, d, dv); }
    const float var = (m2 + dv * (float)(NV * 512)) * (1.0f / GROUP_ELEMS);
    const float rstd = __builtin_amdgcn_rsqf(var + 1e-5f);
    const float dm = mw - mean;
#pragma unroll
    for (int i = 0; i < NV; ++i) {
        const float sc = rstd * gm[i];
        const float sh = fmaf(dm, sc, bt[i]);
        u32x4 o;
#pragma unroll
        for (int j = 0; j < 4; ++j) o[j] = pack_bf16(fmaf(f[i][2 * j], sc, sh), fmaf(f[i][2 * j + 1], sc, sh));
        __builtin_amdgcn_raw_buffer_store_b128(o, ry, voff + i * 1024, soff, ST_AUX);
    }
}

extern "C" void kernel_launch(void* const* d_in, const int* in_sizes, int n_in, void* d_out, int out_size, void* d_ws,
                              size_t ws_size, hipStream_t stream) {
    const int ngroups = in_sizes[0] / GROUP_ELEMS;
    gn_kernel<<<ngroups, TPB, 0, stream>>>(d_in[0], (const uint16_t*)d_in[1], (const uint16_t*)d_in[2], d_out);
}
```

```cpp
#include <hip/hip_runtime.h>
#include <hip/hip_bf16.h>
#include <stdint.h>

typedef unsigned int u32x4 __attribute__((ext_vector_type(4)));
typedef float f32x2 __attribute__((ext_vector_type(2)));
typedef __bf16 bf16x2 __attribute__((ext_vector_type(2)));

template <int CTRL, int ROWMASK>
__device__ __forceinline__ float dpp_add(float v) {
    int t = __builtin_amdgcn_update_dpp(0, __float_as_int(v), CTRL, ROWMASK, 0xf, false);
    return v + __int_as_float(t);
}
__device__ __forceinline__ float wave_sum(float v) {
    v = dpp_add<0xB1, 0xf>(v);
    v = dpp_add<0x4E, 0xf>(v);
    v = dpp_add<0x141, 0xf>(v);
    v = dpp_add<0x140, 0xf>(v);
    v = dpp_add<0x142, 0xa>(v);
    v = dpp_add<0x143, 0xc>(v);
    return __int_as_float(__builtin_amdgcn_readlane(__float_as_int(v), 63));
}
__device__ __forceinline__ float bf_lo(uint32_t u) { return __uint_as_float(u << 16); }
__device__ __forceinline__ float bf_hi(uint32_t u) { return __uint_as_float(u & 0xffff0000u); }
__device__ __forceinline__ uint32_t pack_bf16(f32x2 f) {
    bf16x2 h = __builtin_convertvector(f, bf16x2);
    return __builtin_bit_cast(uint32_t, h);
}
constexpr int GROUP_ELEMS = 16384;
constexpr int NGROUPS = 1024;

template <int TPB, int GRID, int DEPTH, int WHEN, int LD_AUX, int ST_AUX>
__global__ __launch_bounds__(TPB) void gn3(const void* __restrict__ x, const uint16_t* __restrict__ gamma,
                                           const uint16_t* __restrict__ beta, void* __restrict__ y) {
    constexpr int NW = TPB / 64;
    constexpr int NV = GROUP_ELEMS / 8 / TPB;
    constexpr int NG = NGROUPS / GRID;
    constexpr size_t GSTRIDE = (size_t)GRID * GROUP_ELEMS * 2;
    __shared__ f32x2 red[NG][NW];
    const int lane = threadIdx.x & 63;
    const int wid = __builtin_amdgcn_readfirstlane(threadIdx.x >> 6);
    const int voff = lane * 16;
    const size_t wbase = (size_t)blockIdx.x * (GROUP_ELEMS * 2) + (size_t)wid * (NV * 1024);
    const char* px = (const char*)x + wbase;
    char* py = (char*)y + wbase;
    u32x4 v[NG][NV];
    auto load = [&](int g) {
        const __amdgpu_buffer_rsrc_t rx = __builtin_amdgcn_make_buffer_rsrc((void*)(px + g * GSTRIDE), 0, NV * 1024, 0x00020000);
#pragma unroll
        for (int i = 0; i < NV; ++i) v[g][i] = __builtin_amdgcn_raw_buffer_load_b128(rx, voff + i * 1024, 0, LD_AUX);
        __builtin_amdgcn_sched_barrier(0);
    };
#pragma unroll
    for (int g = 0; g < DEPTH && g < NG; ++g) load(g);
    const int c0 = (blockIdx.x & 31) * 32 + wid * NV;
    float gm[NV], bt[NV];
    {
        const uint32_t* g32 = (const uint32_t*)(gamma + c0);
        const uint32_t* b32 = (const uint32_t*)(beta + c0);
#pragma unroll
        for (int i = 0; i < NV / 2; ++i) {
            const uint32_t gg = g32[i], bb = b32[i];
            gm[2 * i] = bf_lo(gg); gm[2 * i + 1] = bf_hi(gg);
            bt[2 * i] = bf_lo(bb); bt[2 * i + 1] = bf_hi(bb);
        }
    }
#pragma unroll
    for (int it = 0; it < NG; ++it) {
        f32x2 f[NV][4];
        f32x2 s = {0.f, 0.f};
#pragma unroll
        for (int i = 0; i < NV; ++i)
#pragma unroll
            for (int j = 0; j < 4; ++j) { f[i][j] = f32x2{bf_lo(v[it][i][j]), bf_hi(v[it][i][j])}; s += f[i][j]; }
        __builtin_amdgcn_sched_barrier(0);
        if (WHEN == 0 && it + DEPTH < NG) load(it + DEPTH);
        const float mw = wave_sum(s.x + s.y) * (1.0f / (NV * 512));
        f32x2 q = {0.f, 0.f};
#pragma unroll
        for (int i = 0; i < NV; ++i)
#pragma unroll
            for (int j = 0; j < 4; ++j) { f[i][j] -= mw; q = __builtin_elementwise_fma(f[i][j], f[i][j], q); }
        const float m2w = wave_sum(q.x + q.y);
        if (lane == 0) red[it][wid] = f32x2{mw, m2w};
        __builtin_amdgcn_s_waitcnt(0xc07f);
        __builtin_amdgcn_s_barrier();
        float mean = 0.f, m2 = 0.f;
#pragma unroll
        for (int w = 0; w < NW; ++w) { mean += red[it][w].x; m2 += red[it][w].y; }
        mean *= (1.0f / NW);
        float dv = 0.f;
#pragma unroll
        for (int w = 0; w < NW; ++w) { float d = red[it][w].x - mean; dv = fmaf(d, d, dv); }
        const float var = (m2 + dv * (float)(NV * 512)) * (1.0f / GROUP_ELEMS);
        const float rstd = __builtin_amdgcn_rsqf(var + 1e-5f);
        const float dm = mw - mean;
        if (WHEN == 1 && it + DEPTH < NG) { __builtin_amdgcn_sched_barrier(0); load(it + DEPTH); }
        const __amdgpu_buffer_rsrc_t ry = __builtin_amdgcn_make_buffer_rsrc((void*)(py + it * GSTRIDE), 0, NV * 1024, 0x00020000);
#pragma unroll
        for (int i = 0; i < NV; ++i) {
            const float sc = rstd * gm[i];
            const float sh = fmaf(dm, sc, bt[i]);
            u32x4 o;
#pragma unroll
            for (int j = 0; j < 4; ++j) o[j] = pack_bf16(__builtin_elementwise_fma(f[i][j], f32x2{sc, sc}, f32x2{sh, sh}));
            __builtin_amdgcn_raw_buffer_store_b128(o, ry, voff + i * 1024, 0, ST_AUX);
        }
        __builtin_amdgcn_sched_barrier(0);
        if (WHEN == 2 && it + DEPTH < NG) load(it + DEPTH);
    }
}

extern "C" void kernel_launch(void* const* d_in, const int* in_sizes, int n_in, void* d_out, int out_size, void* d_ws,
                              size_t ws_size, hipStream_t stream) {
    gn3<1024, 256, 1, 0, 2, 16><<<256, 1024, 0, stream>>>(d_in[0], (const uint16_t*)d_in[1], (const uint16_t*)d_in[2], d_out);
}
```

```cpp
#include <hip/hip_runtime.h>
#include <hip/hip_bf16.h>
#include <stdint.h>

typedef unsigned int u32x4 __attribute__((ext_vector_type(4)));
typedef float f32x2 __attribute__((ext_vector_type(2)));
typedef __bf16 bf16x2 __attribute__((ext_vector_type(2)));

#define TPB 256
#define LD_AUX 2
#define ST_AUX 16
#define PRIO 1
#define NE 128
#define MINW 4

template <int CTRL, int ROWMASK>
__device__ __forceinline__ float dpp_add(float v) {
    int t = __builtin_amdgcn_update_dpp(0, __float_as_int(v), CTRL, ROWMASK, 0xf, false);
    return v + __int_as_float(t);
}
__device__ __forceinline__ float wave_sum(float v) {
    v = dpp_add<0xB1, 0xf>(v);
    v = dpp_add<0x4E, 0xf>(v);
    v = dpp_add<0x141, 0xf>(v);
    v = dpp_add<0x140, 0xf>(v);
    v = dpp_add<0x142, 0xa>(v);
    v = dpp_add<0x143, 0xc>(v);
    return __int_as_float(__builtin_amdgcn_readlane(__float_as_int(v), 63));
}
__device__ __forceinline__ float bf_lo(uint32_t u) { return __uint_as_float(u << 16); }
__device__ __forceinline__ float bf_hi(uint32_t u) { return __uint_as_float(u & 0xffff0000u); }
__device__ __forceinline__ uint32_t pack_bf16(f32x2 f) {
    bf16x2 h = __builtin_convertvector(f, bf16x2);
    return __builtin_bit_cast(uint32_t, h);
}
constexpr int GROUP_ELEMS = 16384;
constexpr int NW = TPB / 64;
constexpr int NV = GROUP_ELEMS / 8 / TPB;
constexpr int NO = 256 - NE;

__global__ __launch_bounds__(TPB, MINW) void gn_kernel(const void* __restrict__ x, const uint16_t* __restrict__ gamma,
                                                       const uint16_t* __restrict__ beta, void* __restrict__ y) {
    __shared__ f32x2 red[NW];
    if (PRIO) __builtin_amdgcn_s_setprio(3);
    int grp = blockIdx.x;
    if (NE != 128 && grp >= NO * 8) {
        if (grp & 1) return;
        grp = NO * 8 + ((grp - NO * 8) >> 1);
    }
    const int lane = threadIdx.x & 63;
    const int wid = __builtin_amdgcn_readfirstlane(threadIdx.x >> 6);
    const int voff = lane * 16;
    const size_t wbase = (size_t)grp * (GROUP_ELEMS * 2) + (size_t)wid * (NV * 1024);
    const __amdgpu_buffer_rsrc_t rx = __builtin_amdgcn_make_buffer_rsrc((char*)x + wbase, 0, NV * 1024, 0x00020000);
    u32x4 v[NV];
#pragma unroll
    for (int i = 0; i < NV; ++i) v[i] = __builtin_amdgcn_raw_buffer_load_b128(rx, voff + i * 1024, 0, LD_AUX);
    __builtin_amdgcn_sched_barrier(0);
    if (PRIO) __builtin_amdgcn_s_setprio(0);
    const int c0 = (grp & 31) * 32 + wid * NV;
    float gm[NV], bt[NV];
    {
        const uint32_t* g32 = (const uint32_t*)(gamma + c0);
        const uint32_t* b32 = (const uint32_t*)(beta + c0);
#pragma unroll
        for (int i = 0; i < NV / 2; ++i) {
            const uint32_t gg = g32[i], bb = b32[i];
            gm[2 * i] = bf_lo(gg); gm[2 * i + 1] = bf_hi(gg);
            bt[2 * i] = bf_lo(bb); bt[2 * i + 1] = bf_hi(bb);
        }
    }
    f32x2 f[NV][4];
    f32x2 s = {0.f, 0.f};
#pragma unroll
    for (int i = 0; i < NV; ++i)
#pragma unroll
        for (int j = 0; j < 4; ++j) { f[i][j] = f32x2{bf_lo(v[i][j]), bf_hi(v[i][j])}; s += f[i][j]; }
    const float mw = wave_sum(s.x + s.y) * (1.0f / (NV * 512));
    f32x2 q = {0.f, 0.f};
#pragma unroll
    for (int i = 0; i < NV; ++i)
#pragma unroll
        for (int j = 0; j < 4; ++j) { f[i][j] -= mw; q = __builtin_elementwise_fma(f[i][j], f[i][j], q); }
    const float m2w = wave_sum(q.x + q.y);
    if (lane == 0) red[wid] = f32x2{mw, m2w};
    __syncthreads();
    float mean = 0.f, m2 = 0.f;
#pragma unroll
    for (int w = 0; w < NW; ++w) { mean += red[w].x; m2 += red[w].y; }
    mean *= (1.0f / NW);
    float dv = 0.f;
#pragma unroll
    for (int w = 0; w < NW; ++w) { float d = red[w].x - mean; dv = fmaf(d, d, dv); }
    const float var = (m2 + dv * (float)(NV * 512)) * (1.0f / GROUP_ELEMS);
    const float rstd = __builtin_amdgcn_rsqf(var + 1e-5f);
    const float dm = mw - mean;
    const __amdgpu_buffer_rsrc_t ry = __builtin_amdgcn_make_buffer_rsrc((char*)y + wbase, 0, NV * 1024, 0x00020000);
#pragma unroll
    for (int i = 0; i < NV; ++i) {
        const float sc = rstd * gm[i];
        const float sh = fmaf(dm, sc, bt[i]);
        u32x4 o;
#pragma unroll
        for (int j = 0; j < 4; ++j) o[j] = pack_bf16(__builtin_elementwise_fma(f[i][j], f32x2{sc, sc}, f32x2{sh, sh}));
        __builtin_amdgcn_raw_buffer_store_b128(o, ry, voff + i * 1024, 0, ST_AUX);
    }
}

extern "C" void kernel_launch(void* const* d_in, const int* in_sizes, int n_in, void* d_out, int out_size, void* d_ws,
                              size_t ws_size, hipStream_t stream) {
    gn_kernel<<<NE * 8, TPB, 0, stream>>>(d_in[0], (const uint16_t*)d_in[1], (const uint16_t*)d_in[2], d_out);
}
```

```cpp
#include <hip/hip_runtime.h>
#include <hip/hip_bf16.h>
#include <stdint.h>

typedef unsigned int u32x4 __attribute__((ext_vector_type(4)));
typedef float f32x2 __attribute__((ext_vector_type(2)));
typedef __bf16 bf16x2 __attribute__((ext_vector_type(2)));

#define TPB 512
#define LD_AUX 2
#define ST_AUX 16
#define LP0 3
#define LP1 3
#define LP2 3
#define LP3 3
#define SP0 0
#define SP1 0
#define SP2 0
#define SP3 0
#define SETP(a, b, c, d) do { const int cls = blockIdx.x >> 8; if (a == b && b == c && c == d) __builtin_amdgcn_s_setprio(a); else if (cls == 0) __builtin_amdgcn_s_setprio(a); else if (cls == 1) __builtin_amdgcn_s_setprio(b); else if (cls == 2) __builtin_amdgcn_s_setprio(c); else __builtin_amdgcn_s_setprio(d); } while (0)
#define NE 128
#define MINW 8

template <int CTRL, int ROWMASK>
__device__ __forceinline__ float dpp_add(float v) {
    int t = __builtin_amdgcn_update_dpp(0, __float_as_int(v), CTRL, ROWMASK, 0xf, false);
    return v + __int_as_float(t);
}
__device__ __forceinline__ float wave_sum(float v) {
    v = dpp_add<0xB1, 0xf>(v);
    v = dpp_add<0x4E, 0xf>(v);
    v = dpp_add<0x141, 0xf>(v);
    v = dpp_add<0x140, 0xf>(v);
    v = dpp_add<0x142, 0xa>(v);
    v = dpp_add<0x143, 0xc>(v);
    return __int_as_float(__builtin_amdgcn_readlane(__float_as_int(v), 63));
}
__device__ __forceinline__ float bf_lo(uint32_t u) { return __uint_as_float(u << 16); }
__device__ __forceinline__ float bf_hi(uint32_t u) { return __uint_as_float(u & 0xffff0000u); }
__device__ __forceinline__ uint32_t pack_bf16(f32x2 f) {
    bf16x2 h = __builtin_convertvector(f, bf16x2);
    return __builtin_bit_cast(uint32_t, h);
}
constexpr int GROUP_ELEMS = 16384;
constexpr int NW = TPB / 64;
constexpr int NV = GROUP_ELEMS / 8 / TPB;
constexpr int NO = 256 - NE;

__global__ __launch_bounds__(TPB, MINW) void gn_kernel(const void* __restrict__ x, const uint16_t* __restrict__ gamma,
                                                       const uint16_t* __restrict__ beta, void* __restrict__ y) {
    __shared__ f32x2 red[NW];
    SETP(LP0, LP1, LP2, LP3);
    int grp = blockIdx.x;
    if (NE != 128 && grp >= NO * 8) {
        if (grp & 1) return;
        grp = NO * 8 + ((grp - NO * 8) >> 1);
    }
    const int lane = threadIdx.x & 63;
    const int wid = __builtin_amdgcn_readfirstlane(threadIdx.x >> 6);
    const int voff = lane * 16;
    const size_t wbase = (size_t)grp * (GROUP_ELEMS * 2) + (size_t)wid * (NV * 1024);
    const __amdgpu_buffer_rsrc_t rx = __builtin_amdgcn_make_buffer_rsrc((char*)x + wbase, 0, NV * 1024, 0x00020000);
    u32x4 v[NV];
#pragma unroll
    for (int i = 0; i < NV; ++i) v[i] = __builtin_amdgcn_raw_buffer_load_b128(rx, voff + i * 1024, 0, LD_AUX);
    __builtin_amdgcn_sched_barrier(0);
    SETP(SP0, SP1, SP2, SP3);
    const int c0 = (grp & 31) * 32 + wid * NV;
    float gm[NV], bt[NV];
    {
        const uint32_t* g32 = (const uint32_t*)(gamma + c0);
        const uint32_t* b32 = (const uint32_t*)(beta + c0);
#pragma unroll
        for (int i = 0; i < NV / 2; ++i) {
            const uint32_t gg = g32[i], bb = b32[i];
            gm[2 * i] = bf_lo(gg); gm[2 * i + 1] = bf_hi(gg);
            bt[2 * i] = bf_lo(bb); bt[2 * i + 1] = bf_hi(bb);
        }
    }
    f32x2 f[NV][4];
    f32x2 s = {0.f, 0.f};
#pragma unroll
    for (int i = 0; i < NV; ++i)
#pragma unroll
        for (int j = 0; j < 4; ++j) { f[i][j] = f32x2{bf_lo(v[i][j]), bf_hi(v[i][j])}; s += f[i][j]; }
    const float mw = wave_sum(s.x + s.y) * (1.0f / (NV * 512));
    f32x2 q = {0.f, 0.f};
#pragma unroll
    for (int i = 0; i < NV; ++i)
#pragma unroll
        for (int j = 0; j < 4; ++j) { f[i][j] -= mw; q = __builtin_elementwise_fma(f[i][j], f[i][j], q); }
    const float m2w = wave_sum(q.x + q.y);
    if (lane == 0) red[wid] = f32x2{mw, m2w};
    __syncthreads();
    float mean = 0.f, m2 = 0.f;
#pragma unroll
    for (int w = 0; w < NW; ++w) { mean += red[w].x; m2 += red[w].y; }
    mean *= (1.0f / NW);
    float dv = 0.f;
#pragma unroll
    for (int w = 0; w < NW; ++w) { float d = red[w].x - mean; dv = fmaf(d, d, dv); }
    const float var = (m2 + dv * (float)(NV * 512)) * (1.0f / GROUP_ELEMS);
    const float rstd = __builtin_amdgcn_rsqf(var + 1e-5f);
    const float dm = mw - mean;
    const __amdgpu_buffer_rsrc_t ry = __builtin_amdgcn_make_buffer_rsrc((char*)y + wbase, 0, NV * 1024, 0x00020000);
#pragma unroll
    for (int i = 0; i < NV; ++i) {
        const float sc = rstd * gm[i];
        const float sh = fmaf(dm, sc, bt[i]);
        u32x4 o;
#pragma unroll
        for (int j = 0; j < 4; ++j) o[j] = pack_bf16(__builtin_elementwise_fma(f[i][j], f32x2{sc, sc}, f32x2{sh, sh}));
        __builtin_amdgcn_raw_buffer_store_b128(o, ry, voff + i * 1024, 0, ST_AUX);
    }
}

extern "C" void kernel_launch(void* const* d_in, const int* in_sizes, int n_in, void* d_out, int out_size, void* d_ws,
                              size_t ws_size, hipStream_t stream) {
    gn_kernel<<<NE * 8, TPB, 0, stream>>>(d_in[0], (const uint16_t*)d_in[1], (const uint16_t*)d_in[2], d_out);
}
```

```cpp
#include <hip/hip_runtime.h>
#include <hip/hip_bf16.h>
#include <stdint.h>

typedef unsigned int u32x4 __attribute__((ext_vector_type(4)));
typedef float f32x2 __attribute__((ext_vector_type(2)));
typedef __bf16 bf16x2 __attribute__((ext_vector_type(2)));

#define TPB 256
#define LD_AUX 2
#define ST_AUX 16
#define PRIO 1
#define NE 128
#define MINW 4

template <int CTRL, int ROWMASK>
__device__ __forceinline__ float dpp_add(float v) {
    int t = __builtin_amdgcn_update_dpp(0, __float_as_int(v), CTRL, ROWMASK, 0xf, false);
    return v + __int_as_float(t);
}
__device__ __forceinline__ float wave_sum(float v) {
    v = dpp_add<0xB1, 0xf>(v);
    v = dpp_add<0x4E, 0xf>(v);
    v = dpp_add<0x141, 0xf>(v);
    v = dpp_add<0x140, 0xf>(v);
    v = dpp_add<0x142, 0xa>(v);
    v = dpp_add<0x143, 0xc>(v);
    return __int_as_float(__builtin_amdgcn_readlane(__float_as_int(v), 63));
}
__device__ __forceinline__ float bf_lo(uint32_t u) { return __uint_as_float(u << 16); }
__device__ __forceinline__ float bf_hi(uint32_t u) { return __uint_as_float(u & 0xffff0000u); }
__device__ __forceinline__ uint32_t pack_bf16(f32x2 f) {
    bf16x2 h = __builtin_convertvector(f, bf16x2);
    return __builtin_bit_cast(uint32_t, h);
}
constexpr int GROUP_ELEMS = 16384;
constexpr int NW = TPB / 64;
constexpr int NV = GROUP_ELEMS / 8 / TPB;
constexpr int NO = 256 - NE;

__global__ __launch_bounds__(TPB, MINW) void gn_kernel(const void* __restrict__ x, const uint16_t* __restrict__ gamma,
                                                       const uint16_t* __restrict__ beta, void* __restrict__ y) {
    __shared__ f32x2 red[NW];
    if (PRIO) __builtin_amdgcn_s_setprio(3);
    int grp = blockIdx.x;
    if (NE != 128 && grp >= NO * 8) {
        if (grp & 1) return;
        grp = NO * 8 + ((grp - NO * 8) >> 1);
    }
    const int lane = threadIdx.x & 63;
    const int wid = __builtin_amdgcn_readfirstlane(threadIdx.x >> 6);
    const int voff = lane * 16;
    const size_t wbase = (size_t)grp * (GROUP_ELEMS * 2) + (size_t)wid * (NV * 1024);
    const __amdgpu_buffer_rsrc_t rx = __builtin_amdgcn_make_buffer_rsrc((char*)x + wbase, 0, NV * 1024, 0x00020000);
    u32x4 v[NV];
#pragma unroll
    for (int i = 0; i < NV; ++i) v[i] = __builtin_amdgcn_raw_buffer_load_b128(rx, voff + i * 1024, 0, LD_AUX);
    __builtin_amdgcn_sched_barrier(0);
    if (PRIO) __builtin_amdgcn_s_setprio(0);
    const int c0 = (grp & 31) * 32 + wid * NV;
    float gm[NV], bt[NV];
    {
        const uint32_t* g32 = (const uint32_t*)(gamma + c0);
        const uint32_t* b32 = (const uint32_t*)(beta + c0);
#pragma unroll
        for (int i = 0; i < NV / 2; ++i) {
            const uint32_t gg = g32[i], bb = b32[i];
            gm[2 * i] = bf_lo(gg); gm[2 * i + 1] = bf_hi(gg);
            bt[2 * i] = bf_lo(bb); bt[2 * i + 1] = bf_hi(bb);
        }
    }
    f32x2 f[NV][4];
    f32x2 s = {0.f, 0.f};
#pragma unroll
    for (int i = 0; i < NV; ++i)
#pragma unroll
        for (int j = 0; j < 4; ++j) { f[i][j] = f32x2{bf_lo(v[i][j]), bf_hi(v[i][j])}; s += f[i][j]; }
    const float mw = wave_sum(s.x + s.y) * (1.0f / (NV * 512));
    f32x2 q = {0.f, 0.f};
#pragma unroll
    for (int i = 0; i < NV; ++i)
#pragma unroll
        for (int j = 0; j < 4; ++j) { f[i][j] -= mw; q = __builtin_elementwise_fma(f[i][j], f[i][j], q); }
    const float m2w = wave_sum(q.x + q.y);
    if (lane == 0) red[wid] = f32x2{mw, m2w};
    __syncthreads();
    float mean = 0.f, m2 = 0.f;
#pragma unroll
    for (int w = 0; w < NW; ++w) { mean += red[w].x; m2 += red[w].y; }
    mean *= (1.0f / NW);
    float dv = 0.f;
#pragma unroll
    for (int w = 0; w < NW; ++w) { float d = red[w].x - mean; dv = fmaf(d, d, dv); }
    const float var = (m2 + dv * (float)(NV * 512)) * (1.0f / GROUP_ELEMS);
    const float rstd = __builtin_amdgcn_rsqf(var + 1e-5f);
    const float dm = mw - mean;
    const __amdgpu_buffer_rsrc_t ry = __builtin_amdgcn_make_buffer_rsrc((char*)y + wbase, 0, NV * 1024, 0x00020000);
#pragma unroll
    for (int i = 0; i < NV; ++i) {
        const float sc = rstd * gm[i];
        const float sh = fmaf(dm, sc, bt[i]);
        u32x4 o;
#pragma unroll
        for (int j = 0; j < 4; ++j) o[j] = pack_bf16(__builtin_elementwise_fma(f[i][j], f32x2{sc, sc}, f32x2{sh, sh}));
        __builtin_amdgcn_raw_buffer_store_b128(o, ry, voff + i * 1024, 0, ST_AUX);
    }
}

typedef unsigned long long u64;
__global__ __launch_bounds__(1024) void first_kernel(u64* ts) {
    u64 t0 = __builtin_amdgcn_s_memrealtime();
    if ((threadIdx.x & 63) == 0) ts[blockIdx.x * (1024 / 64) + (threadIdx.x >> 6)] = t0;
}
__global__ __launch_bounds__(1024) void probe_kernel(const u64* ts) {
    __shared__ u64 mn0;
    if (threadIdx.x == 0) mn0 = ~0ull;
    __syncthreads();
    const int n = 256 * (1024 / 64);
    u64 m = ~0ull;
    for (int i = threadIdx.x; i < n; i += 1024) m = min(m, ts[i]);
    atomicMin(&mn0, m);
    __syncthreads();
    if (threadIdx.x == 0) {
        const u64 target = mn0 + 20000;
        for (int it = 0; it < 20000000; ++it) { if (__builtin_amdgcn_s_memrealtime() >= target) break; __builtin_amdgcn_s_sleep(1); }
    }
}
extern "C" void kernel_launch(void* const* d_in, const int* in_sizes, int n_in, void* d_out, int out_size, void* d_ws,
                              size_t ws_size, hipStream_t stream) {
    first_kernel<<<256, 1024, 0, stream>>>((u64*)d_ws);
    gn_kernel<<<NE * 8, TPB, 0, stream>>>(d_in[0], (const uint16_t*)d_in[1], (const uint16_t*)d_in[2], d_out);
    probe_kernel<<<1, 1024, 0, stream>>>((const u64*)d_ws);
}
```

```cpp
#include <hip/hip_runtime.h>
#include <hip/hip_bf16.h>
#include <stdint.h>

typedef unsigned int u32x4 __attribute__((ext_vector_type(4)));
typedef float f32x2 __attribute__((ext_vector_type(2)));
typedef __bf16 bf16x2 __attribute__((ext_vector_type(2)));

constexpr int TPB = 256;
constexpr int GROUP_ELEMS = 16384;
constexpr int NGROUPS = 32 * 32;
constexpr int NW = TPB / 64;
constexpr int NV = GROUP_ELEMS / 8 / TPB;
constexpr int CH_BYTES = 1024;
constexpr int CS = NW * CH_BYTES;
constexpr int WAVE_RANGE = (NV - 1) * CS + CH_BYTES;
constexpr int LD_AUX = 2;
constexpr int ST_AUX = 16;

template <int CTRL, int ROWMASK>
__device__ __forceinline__ float dpp_add(float v) {
    int t = __builtin_amdgcn_update_dpp(0, __float_as_int(v), CTRL, ROWMASK, 0xf, false);
    return v + __int_as_float(t);
}
__device__ __forceinline__ float wave_sum(float v) {
    v = dpp_add<0xB1, 0xf>(v);
    v = dpp_add<0x4E, 0xf>(v);
    v = dpp_add<0x141, 0xf>(v);
    v = dpp_add<0x140, 0xf>(v);
    v = dpp_add<0x142, 0xa>(v);
    v = dpp_add<0x143, 0xc>(v);
    return __int_as_float(__builtin_amdgcn_readlane(__float_as_int(v), 63));
}
__device__ __forceinline__ float bf_lo(uint32_t u) { return __uint_as_float(u << 16); }
__device__ __forceinline__ float bf_hi(uint32_t u) { return __uint_as_float(u & 0xffff0000u); }
__device__ __forceinline__ uint32_t pack_bf16(f32x2 f) {
    bf16x2 h = __builtin_convertvector(f, bf16x2);
    return __builtin_bit_cast(uint32_t, h);
}

__global__ __launch_bounds__(TPB, 4) void gn_kernel(const void* __restrict__ x, const uint16_t* __restrict__ gamma,
                                                    const uint16_t* __restrict__ beta, void* __restrict__ y) {
    __shared__ f32x2 red[NW];
    __builtin_amdgcn_s_setprio(3);
    const int grp = blockIdx.x;
    const int lane = threadIdx.x & 63;
    const int wid = __builtin_amdgcn_readfirstlane(threadIdx.x >> 6);
    const int voff = lane * 16;
    const size_t wbase = (size_t)grp * (GROUP_ELEMS * 2) + (size_t)wid * CH_BYTES;
    const __amdgpu_buffer_rsrc_t rx = __builtin_amdgcn_make_buffer_rsrc((char*)x + wbase, 0, WAVE_RANGE, 0x00020000);
    u32x4 v[NV];
#pragma unroll
    for (int i = 0; i < NV; ++i) v[i] = __builtin_amdgcn_raw_buffer_load_b128(rx, voff + i * CS, 0, LD_AUX);
    __builtin_amdgcn_sched_barrier(0);
    __builtin_amdgcn_s_setprio(0);

    float gm[NV], bt[NV];
    {
        const int c0 = (grp & 31) * 32 + (wid & 2);
        const uint32_t* g32 = (const uint32_t*)(gamma + c0);
        const uint32_t* b32 = (const uint32_t*)(beta + c0);
        const int sh = (wid & 1) ? 0 : 16;
#pragma unroll
        for (int i = 0; i < NV; ++i) {
            gm[i] = __uint_as_float((g32[2 * i] << sh) & 0xffff0000u);
            bt[i] = __uint_as_float((b32[2 * i] << sh) & 0xffff0000u);
        }
    }

    f32x2 f[NV][4];
    f32x2 s = {0.f, 0.f};
#pragma unroll
    for (int i = 0; i < NV; ++i)
#pragma unroll
        for (int j = 0; j < 4; ++j) { f[i][j] = f32x2{bf_lo(v[i][j]), bf_hi(v[i][j])}; s += f[i][j]; }
    const float mw = wave_sum(s.x + s.y) * (1.0f / (NV * 512));
    f32x2 q = {0.f, 0.f};
#pragma unroll
    for (int i = 0; i < NV; ++i)
#pragma unroll
        for (int j = 0; j < 4; ++j) { f[i][j] -= mw; q = __builtin_elementwise_fma(f[i][j], f[i][j], q); }
    const float m2w = wave_sum(q.x + q.y);
    if (lane == 0) red[wid] = f32x2{mw, m2w};
    __syncthreads();
    float mean = 0.f, m2 = 0.f;
#pragma unroll
    for (int w = 0; w < NW; ++w) { mean += red[w].x; m2 += red[w].y; }
    mean *= (1.0f / NW);
    float dv = 0.f;
#pragma unroll
    for (int w = 0; w < NW; ++w) { float d = red[w].x - mean; dv = fmaf(d, d, dv); }
    const float var = (m2 + dv * (float)(NV * 512)) * (1.0f / GROUP_ELEMS);
    const float rstd = __builtin_amdgcn_rsqf(var + 1e-5f);
    const float dm = mw - mean;

    const __amdgpu_buffer_rsrc_t ry = __builtin_amdgcn_make_buffer_rsrc((char*)y + wbase, 0, WAVE_RANGE, 0x00020000);
    asm volatile("" ::: "v100");
#pragma unroll
    for (int i = 0; i < NV; ++i) {
        const float sc = rstd * gm[i];
        const float sh = fmaf(dm, sc, bt[i]);
        u32x4 o;
#pragma unroll
        for (int j = 0; j < 4; ++j) o[j] = pack_bf16(__builtin_elementwise_fma(f[i][j], f32x2{sc, sc}, f32x2{sh, sh}));
        __builtin_amdgcn_raw_buffer_store_b128(o, ry, voff + i * CS, 0, ST_AUX);
    }
}

extern "C" void kernel_launch(void* const* d_in, const int* in_sizes, int n_in, void* d_out, int out_size, void* d_ws,
                              size_t ws_size, hipStream_t stream) {
    gn_kernel<<<NGROUPS, TPB, 0, stream>>>(d_in[0], (const uint16_t*)d_in[1], (const uint16_t*)d_in[2], d_out);
}
```
